```python
import torch

def _softmax(x, axis=-1):
    return torch.softmax(x, dim=axis)
def _gelu(x):
    return torch.nn.functional.gelu(x, approximate="tanh")
def _silu(x):
    return torch.nn.functional.silu(x)
def _sigmoid(x):
    return torch.sigmoid(x)
def _layernorm(x, axis=-1, eps=1e-5):
    m = x.mean(dim=axis, keepdim=True)
    v = x.var(dim=axis, keepdim=True, unbiased=False)
    return (x - m) / torch.sqrt(v + eps)
def _scatter_add(tgt, idx, src, axis=0):
    return tgt.clone().index_add_(axis, idx, src)
def _segment_sum(src, seg, n):
    out = torch.zeros((n,) + src.shape[1:], dtype=src.dtype)
    return out.index_add_(0, seg, src)
def _pad(x, pw):
    flat = tuple(v for pair in reversed(pw) for v in pair)
    return torch.nn.functional.pad(x, flat)

kernel_name = 'concat_heads__gqa_block_cached__gqa_block_cached__near_ridge'

def setup_inputs():

    _w = (lambda a: (lambda t: t.to(torch.bfloat16) if t.is_floating_point() else t)(torch.as_tensor(a)))
    def _A_setup():
        B, Sq, D = 8, 112, 576
        Hq, Hkv, Sk, Dh = 32, 16, 512, 128
        assert Hq % Hkv == 0, (Hq, Hkv)
        return {
            "x": _w((lambda *s: torch.randn(*s, dtype=torch.bfloat16) * 0.8)(B, Sq, D)),
            "k_cache": _w((lambda *s: torch.randn(*s, dtype=torch.bfloat16) * 0.8)(B, Hkv, Sk, Dh)),
            "v_cache": _w((lambda *s: torch.randn(*s, dtype=torch.bfloat16) * 0.05)(B, Hkv, Sk, Dh)),
            "wq": _w((lambda *s: torch.randn(*s, dtype=torch.bfloat16) * 0.8)(D, Hq * Dh) * (D ** -0.5)),
            "wo": _w((lambda *s: torch.randn(*s, dtype=torch.bfloat16) * 0.05)(Hq * Dh, D) * ((Hq * Dh) ** -0.5)),
        }
    _A_in = _A_setup()

    def _B_setup():
        B, Sq, D = 8, 112, 576
        Hq, Hkv, Sk, Dh = 32, 16, 512, 128
        assert Hq % Hkv == 0, (Hq, Hkv)
        return {
            "x": _w((lambda *s: torch.randn(*s, dtype=torch.bfloat16) * 0.8)(B, Sq, D)),
            "k_cache": _w((lambda *s: torch.randn(*s, dtype=torch.bfloat16) * 0.8)(B, Hkv, Sk, Dh)),
            "v_cache": _w((lambda *s: torch.randn(*s, dtype=torch.bfloat16) * 0.05)(B, Hkv, Sk, Dh)),
            "wq": _w((lambda *s: torch.randn(*s, dtype=torch.bfloat16) * 0.8)(D, Hq * Dh) * (D ** -0.5)),
            "wo": _w((lambda *s: torch.randn(*s, dtype=torch.bfloat16) * 0.05)(Hq * Dh, D) * ((Hq * Dh) ** -0.5)),
        }
    _B_in = _B_setup()

    return {
        "A_x": _A_in["x"],
        "A_k_cache": _A_in["k_cache"],
        "A_v_cache": _A_in["v_cache"],
        "A_wq": _A_in["wq"],
        "A_wo": _A_in["wo"],
        "B_x": _B_in["x"],
        "B_k_cache": _B_in["k_cache"],
        "B_v_cache": _B_in["v_cache"],
        "B_wq": _B_in["wq"],
        "B_wo": _B_in["wo"],
    }

def reference(A_x, A_k_cache, A_v_cache, A_wq, A_wo, B_x, B_k_cache, B_v_cache, B_wq, B_wo):
    def _A_ref(x, k_cache, v_cache, wq, wo):
        B, Sq, D = x.shape
        Hq, Dh = 32, 128
        Hkv = k_cache.shape[1]
        rep = Hq // Hkv
        q = (lambda x, *d: x.permute(*d))((x @ wq).reshape(B, Sq, Hq, Dh), 0, 2, 1, 3)
        k = (lambda x, n, axis: torch.repeat_interleave(x, n, dim=axis))(k_cache, rep, axis=1)
        v = (lambda x, n, axis: torch.repeat_interleave(x, n, dim=axis))(v_cache, rep, axis=1)
        scores = torch.einsum("bhqd,bhkd->bhqk", q, k) * (Dh ** -0.5)
        p = _softmax(scores, axis=-1)
        o = torch.einsum("bhqk,bhkd->bhqd", p, v)
        return (lambda x, *d: x.permute(*d))(o, 0, 2, 1, 3).reshape(B, Sq, Hq * Dh) @ wo

    def _B_ref(x, k_cache, v_cache, wq, wo):
        B, Sq, D = x.shape
        Hq, Dh = 32, 128
        Hkv = k_cache.shape[1]
        rep = Hq // Hkv
        q = (lambda x, *d: x.permute(*d))((x @ wq).reshape(B, Sq, Hq, Dh), 0, 2, 1, 3)
        k = (lambda x, n, axis: torch.repeat_interleave(x, n, dim=axis))(k_cache, rep, axis=1)
        v = (lambda x, n, axis: torch.repeat_interleave(x, n, dim=axis))(v_cache, rep, axis=1)
        scores = torch.einsum("bhqd,bhkd->bhqk", q, k) * (Dh ** -0.5)
        p = _softmax(scores, axis=-1)
        o = torch.einsum("bhqk,bhkd->bhqd", p, v)
        return (lambda x, *d: x.permute(*d))(o, 0, 2, 1, 3).reshape(B, Sq, Hq * Dh) @ wo

    a = _A_ref(A_x, A_k_cache, A_v_cache, A_wq, A_wo)
    b = _B_ref(B_x, B_k_cache, B_v_cache, B_wq, B_wo)
    return torch.concatenate([a, b], axis=-1)
```

```cpp
#include <hip/hip_runtime.h>
#include <hip/hip_bf16.h>
#include <stdint.h>

typedef __bf16 bf16x8 __attribute__((ext_vector_type(8)));
typedef __bf16 bf16x2 __attribute__((ext_vector_type(2)));
typedef short s16x4 __attribute__((ext_vector_type(4)));
typedef float f32x2 __attribute__((ext_vector_type(2)));
typedef float f32x4 __attribute__((ext_vector_type(4)));
typedef float f32x16 __attribute__((ext_vector_type(16)));
typedef unsigned u32x2 __attribute__((ext_vector_type(2)));
typedef unsigned u32x4 __attribute__((ext_vector_type(4)));
#define LDS_AS __attribute__((address_space(3)))
#define DI __device__ __forceinline__

constexpr int NB = 8, SQ = 112, DM = 576, HKV = 16, SK = 512, DH = 128, NQ = 4096;
constexpr unsigned SLOT = 16384;
constexpr unsigned LDS_BYTES = 10 * SLOT;
constexpr unsigned QB = 6 * SLOT;
constexpr unsigned OB = 1 * SLOT;
constexpr unsigned PART_BYTES = 129024;
constexpr float QSCALE = 0.08838834764831845f * 1.4426950408889634f;

struct Params {
  const char* x[2]; const char* k[2]; const char* v[2]; const char* wq[2]; const char* wo[2];
  char* part; char* out;
};

DI void dma16(const char* sbase, unsigned voff, unsigned lds_dst) {
  unsigned keep;
  asm volatile("s_mov_b32 %0, m0\n\ts_mov_b32 m0, %3\n\ts_nop 2\n\tglobal_load_lds_dwordx4 %1, %2\n\ts_mov_b32 m0, %0"
               : "=&s"(keep) : "v"(voff), "s"(sbase), "s"(lds_dst) : "memory");
}
DI s16x4 lds_tr(unsigned a) { return __builtin_amdgcn_ds_read_tr16_b64_v4i16((LDS_AS s16x4*)(a)); }
DI bf16x8 lds_tr2(unsigned a0, unsigned a1) {
  s16x4 lo = lds_tr(a0), hi = lds_tr(a1);
  return __builtin_bit_cast(bf16x8, __builtin_shufflevector(lo, hi, 0, 1, 2, 3, 4, 5, 6, 7));
}
DI bf16x8 lds_b128(unsigned a) { return *(const LDS_AS bf16x8*)(a); }
DI unsigned cvtpk(float lo, float hi) {
  f32x2 v = {lo, hi};
  return __builtin_bit_cast(unsigned, __builtin_convertvector(v, bf16x2));
}
DI void lds_w64(unsigned a, unsigned lo, unsigned hi) { u32x2 v = {lo, hi}; *(LDS_AS u32x2*)(a) = v; }
DI float swap32_max(float v) {
  auto r = __builtin_amdgcn_permlane32_swap(__float_as_uint(v), __float_as_uint(v), false, false);
  return fmaxf(__uint_as_float(r[0]), __uint_as_float(r[1]));
}
DI float swap32_add(float v) {
  auto r = __builtin_amdgcn_permlane32_swap(__float_as_uint(v), __float_as_uint(v), false, false);
  return __uint_as_float(r[0]) + __uint_as_float(r[1]);
}
#define MFMA16(a, b, c) __builtin_amdgcn_mfma_f32_16x16x32_bf16(a, b, c, 0, 0, 0)
#define MFMA32(a, b, c) __builtin_amdgcn_mfma_f32_32x32x16_bf16(a, b, c, 0, 0, 0)
#define WAIT_BAR(N) asm volatile("s_waitcnt vmcnt(" #N ") lgkmcnt(0)\n\ts_barrier" ::: "memory")
#define LBAR() asm volatile("s_waitcnt lgkmcnt(0)\n\ts_barrier" ::: "memory")

struct Frags { bf16x8 w[4]; bf16x8 x[4]; };

DI void load_w(Frags& f, unsigned wa) {
#pragma unroll
  for (int a = 0; a < 4; ++a) f.w[a] = lds_tr2(wa + a * 256, wa + a * 256 + 4096);
}
template <int PSTRIDE> DI void load_x(Frags& f, unsigned xa, bool four) {
#pragma unroll
  for (int p = 0; p < 3; ++p) f.x[p] = lds_b128(xa + p * PSTRIDE);
  if (four) f.x[3] = lds_b128(xa + 3 * PSTRIDE);
}
DI void mma_step(f32x4 (&acc)[4][4], const Frags& f, bool four) {
#pragma unroll
  for (int p = 0; p < 3; ++p)
#pragma unroll
    for (int a = 0; a < 4; ++a) acc[a][p] = MFMA16(f.w[a], f.x[p], acc[a][p]);
  if (four) {
#pragma unroll
    for (int a = 0; a < 4; ++a) acc[a][3] = MFMA16(f.w[a], f.x[3], acc[a][3]);
  }
}

__global__ __launch_bounds__(512, 2) void fused_kernel(Params P) {
  extern __shared__ __attribute__((aligned(16))) char smem[];
  const unsigned L0 = (unsigned)(uintptr_t)(LDS_AS char*)smem;
  const int tid = threadIdx.x, lane = tid & 63;
  const int w = __builtin_amdgcn_readfirstlane(tid >> 6);
  const int bid = blockIdx.x, xcd = bid & 7, idx = bid >> 3;
  const int br = xcd >> 2, kvh = 4 * (xcd & 3) + (idx & 3), b = idx >> 2;

  const char* xg = (br ? P.x[1] : P.x[0]) + (size_t)b * SQ * DM * 2;
  const char* wqg = (br ? P.wq[1] : P.wq[0]) + (size_t)kvh * 256 * 2;
  const char* kg = (br ? P.k[1] : P.k[0]) + (size_t)(b * HKV + kvh) * SK * DH * 2;
  const char* vg = (br ? P.v[1] : P.v[0]) + (size_t)(b * HKV + kvh) * SK * DH * 2;
  const char* wog = (br ? P.wo[1] : P.wo[0]) + (size_t)kvh * 256 * DM * 2;

  const int xr0 = min(16 * w + (lane & 7), SQ - 1), xr1 = min(16 * w + 8 + (lane & 7), SQ - 1);
  const unsigned vX0 = xr0 * (DM * 2) + (lane >> 3) * 16, vX1 = xr1 * (DM * 2) + (lane >> 3) * 16;
  const int r8 = (lane >> 1) & 7;
  const int wc = 16 * (w >> 2) + 4 * ((w >> 1) & 1) + (r8 & 3) + 8 * (r8 >> 2);
  const unsigned wcol = (8 * (w & 1) + (lane >> 4)) * 32 + (lane & 1) * 16;
  const unsigned vWq = wc * (NQ * 2) + wcol, vWo = wc * (DM * 2) + wcol;
  const unsigned vK = (8 * w + (lane & 7)) * 256 + (((lane >> 3) ^ (w & 1)) * 16);
  const unsigned vV = (8 * w + ((lane >> 2) & 7)) * 256 + (lane >> 5) * 64 + (lane & 3) * 16;
  const unsigned vW3 = (32 * (w >> 1) + 16 * (w & 1) + (r8 & 3) + 8 * (r8 >> 2)) * (DM * 2) + (lane >> 4) * 32 + (lane & 1) * 16;
  const unsigned dst = L0 + w * 2048;

#define ISSUE_X(s, slot) do { dma16(xg + (s) * 128, vX0, dst + (slot) * SLOT); dma16(xg + (s) * 128, vX1, dst + (slot) * SLOT + 1024); } while (0)
#define ISSUE_WQ(u, slot) do { dma16(wqg + (size_t)(u) * 32 * NQ * 2, vWq, dst + (slot) * SLOT); dma16(wqg + (size_t)(u) * 32 * NQ * 2 + 128, vWq, dst + (slot) * SLOT + 1024); } while (0)
#define ISSUE_K(t, slot) do { dma16(kg + (t) * 16384, vK, dst + (slot) * SLOT); dma16(kg + (t) * 16384 + 128, vK, dst + (slot) * SLOT + 1024); } while (0)
#define ISSUE_V(t, slot) do { dma16(vg + (t) * 16384, vV, dst + (slot) * SLOT); dma16(vg + (t) * 16384 + 128, vV, dst + (slot) * SLOT + 1024); } while (0)
#define ISSUE_WO(u, slot) do { const char* s_ = wog + ((u) & 7) * 32 * DM * 2 + ((u) >> 3) * 512; \
    dma16(s_, vWo, dst + (slot) * SLOT); dma16(s_ + 128, vWo, dst + (slot) * SLOT + 1024); } while (0)
#define ISSUE_WO3(u, slot) do { const char* s_ = wog + ((u) - 16) * 128 * DM * 2 + 1024; \
    dma16(s_, vW3, dst + (slot) * SLOT); dma16(s_ + 4 * DM * 2, vW3, dst + (slot) * SLOT + 1024); } while (0)

  const int nq = w & 3, mh = w >> 2, p0 = mh * 4;
  const bool four = (mh == 0);
  const int g4 = lane >> 4, r16 = lane & 15, tq = (lane & 15) >> 2, tp = lane & 3;
  const unsigned xrd = L0 + (r16 >> 3) * 1024 + g4 * 128 + (r16 & 7) * 16 + p0 * 2048;
  const unsigned wrd = L0 + (g4 >> 1) * 8192 + ((g4 & 1) * 4 + tq) * 32 + 8 * tp + nq * 1024;

  {
    f32x4 acc[4][4];
#pragma unroll
    for (int a = 0; a < 4; ++a)
#pragma unroll
      for (int p = 0; p < 4; ++p) acc[a][p] = f32x4{0.f, 0.f, 0.f, 0.f};
#pragma unroll
    for (int s = 0; s < 3; ++s) { ISSUE_X(s, 3 * s); ISSUE_WQ(2 * s, 3 * s + 1); ISSUE_WQ(2 * s + 1, 3 * s + 2); }
    WAIT_BAR(12);
    Frags fa, fb;
    load_w(fa, wrd + 1 * SLOT); load_x<2048>(fa, xrd, four);
    unsigned sb = 0;
    for (int s = 0; s < 8; ++s) {
      load_w(fb, wrd + sb + 2 * SLOT); load_x<2048>(fb, xrd + sb + 512, four);
      mma_step(acc, fa, four);
      WAIT_BAR(6);
      const int sl = sb / SLOT;
      if (s < 6) { ISSUE_X(s + 3, sl); ISSUE_WQ(2 * s + 6, sl + 1); ISSUE_WQ(2 * s + 7, sl + 2); }
      else if (s == 6) { ISSUE_K(0, 0); ISSUE_V(0, 1); ISSUE_K(1, 2); }
      else { ISSUE_V(1, 3); ISSUE_K(2, 4); ISSUE_V(2, 5); }
      sb = (sb == 6 * SLOT) ? 0 : sb + 3 * SLOT;
      load_w(fa, wrd + sb + 1 * SLOT); load_x<2048>(fa, xrd + sb, four);
      mma_step(acc, fb, four);
    }
    load_w(fb, wrd + 8 * SLOT); load_x<2048>(fb, xrd + 6 * SLOT + 512, four);
    mma_step(acc, fa, four);
    LBAR();
    mma_step(acc, fb, four);
    const unsigned qw = L0 + QB + ((nq >> 1) * 14 + 2 * p0 + (r16 >> 3)) * 2048 + (r16 & 7) * 16 + 8 * (g4 & 1);
#pragma unroll
    for (int a = 0; a < 4; ++a) {
      const unsigned ch = (unsigned)((8 * (nq & 1) + 2 * a + (g4 >> 1)) ^ (r16 >> 3)) * 128;
#pragma unroll
      for (int p = 0; p < 4; ++p) {
        if (p < 3 || four) {
          const f32x4 c = acc[a][p] * QSCALE;
          lds_w64(qw + ch + p * 4096, cvtpk(c[0], c[1]), cvtpk(c[2], c[3]));
        }
      }
    }
  }
  LBAR();

  {
    const int r32 = lane & 31, h = lane >> 5;
    const unsigned krd0 = (r32 >> 3) * 2048 + (unsigned)(h ^ ((r32 >> 3) & 1)) * 128 + (r32 & 7) * 16;
    const unsigned krd = L0 + krd0;
    const unsigned vrd = L0 + (4 * h + tq) * 64 + 32 * ((lane >> 4) & 1) + 8 * tp;
    const bool act = (w < 7), grpA = (w < 4);
    bf16x8 qf[8];
    {
      const unsigned qa = L0 + QB + (act ? w : 0) * 8192 + krd0;
#pragma unroll
      for (int s = 0; s < 8; ++s) qf[s] = lds_b128(qa + s * 256);
    }
    f32x16 o[4];
#pragma unroll
    for (int d = 0; d < 4; ++d)
#pragma unroll
      for (int r = 0; r < 16; ++r) o[d][r] = 0.f;
    float m = -INFINITY, l = 0.f;
    u32x4 pw[4];
#pragma unroll
    for (int i = 0; i < 4; ++i) pw[i] = u32x4{0u, 0u, 0u, 0u};

#define PV_STEP(vbase) do { _Pragma("unroll") for (int db = 0; db < 4; ++db) { _Pragma("unroll") for (int ks = 0; ks < 4; ++ks) { \
      const bf16x8 vf = lds_tr2((vbase) + ks * 4096 + db * 512, (vbase) + ks * 4096 + 2048 + db * 512); \
      o[db] = MFMA32(vf, __builtin_bit_cast(bf16x8, pw[ks]), o[db]); } } } while (0)

    unsigned ksl = 0;
    unsigned vprev = 0;
    for (int t = 0; t < 8; ++t) {
      WAIT_BAR(8);
      if (t == 0) { ISSUE_K(3, 6); ISSUE_V(3, 7); ISSUE_K(4, 8); ISSUE_V(4, 9); }
      else if (t == 1) { ISSUE_K(5, 0); }
      else if (t == 2) { ISSUE_K(6, 2); ISSUE_V(5, 1); }
      else if (t == 3) { ISSUE_K(7, 4); ISSUE_V(6, 3); }
      else if (t == 4) { ISSUE_V(7, 5); ISSUE_WO(0, 6); }
      else if (t == 5) { ISSUE_WO(1, 8); ISSUE_WO(2, 7); }
      else if (t == 6) { ISSUE_WO(3, 0); ISSUE_WO(4, 9); }
      if (act) {
        if (!grpA && t > 0) PV_STEP(vrd + vprev);
        f32x16 s0, s1;
#pragma unroll
        for (int r = 0; r < 16; ++r) { s0[r] = 0.f; s1[r] = 0.f; }
        const unsigned ka = krd + ksl;
#pragma unroll
        for (int s = 0; s < 8; ++s) {
          const bf16x8 k0 = lds_b128(ka + s * 256), k1 = lds_b128(ka + s * 256 + 8192);
          s0 = MFMA32(k0, qf[s], s0);
          s1 = MFMA32(k1, qf[s], s1);
        }
        float tmax = fmaxf(s0[0], s1[0]);
#pragma unroll
        for (int r = 1; r < 16; ++r) tmax = fmaxf(tmax, fmaxf(s0[r], s1[r]));
        tmax = swap32_max(tmax);
        const float mnew = fmaxf(m, tmax);
        const float alpha = __builtin_amdgcn_exp2f(m - mnew);
        m = mnew;
        float ps = 0.f;
#pragma unroll
        for (int r = 0; r < 16; ++r) { s0[r] = __builtin_amdgcn_exp2f(s0[r] - mnew); ps += s0[r]; }
#pragma unroll
        for (int r = 0; r < 16; ++r) { s1[r] = __builtin_amdgcn_exp2f(s1[r] - mnew); ps += s1[r]; }
        l = l * alpha + ps;
        if (__any(alpha < 1.f)) {
#pragma unroll
          for (int d = 0; d < 4; ++d)
#pragma unroll
            for (int r = 0; r < 16; ++r) o[d][r] *= alpha;
        }
#pragma unroll
        for (int i = 0; i < 4; ++i) {
          pw[0][i] = cvtpk(s0[2 * i], s0[2 * i + 1]);
          pw[1][i] = cvtpk(s0[8 + 2 * i], s0[8 + 2 * i + 1]);
          pw[2][i] = cvtpk(s1[2 * i], s1[2 * i + 1]);
          pw[3][i] = cvtpk(s1[8 + 2 * i], s1[8 + 2 * i + 1]);
        }
        if (grpA) PV_STEP(vrd + ksl + SLOT);
      }
      vprev = ksl + SLOT;
      ksl = (ksl == 8 * SLOT) ? 0 : ksl + 2 * SLOT;
    }
    LBAR();
    if (act) {
      if (!grpA) PV_STEP(vrd + vprev);
      const float inv = 1.f / swap32_add(l);
      const unsigned ow = L0 + OB + (4 * w + (r32 >> 3)) * 2048 + (r32 & 7) * 16 + 8 * h;
#pragma unroll
      for (int db = 0; db < 4; ++db)
#pragma unroll
        for (int i = 0; i < 4; ++i)
          lds_w64(ow + (4 * db + i) * 128, cvtpk(o[db][4 * i] * inv, o[db][4 * i + 1] * inv),
                  cvtpk(o[db][4 * i + 2] * inv, o[db][4 * i + 3] * inv));
    }
#undef PV_STEP
  }
  WAIT_BAR(6);
  ISSUE_WO(5, 5);

  {
    char* part = P.part + (size_t)((br * NB + b) * HKV + kvh) * PART_BYTES;
    const unsigned ord = L0 + OB + (r16 >> 3) * 2048 + g4 * 128 + (r16 & 7) * 16 + p0 * 4096;
    constexpr unsigned ring[6] = {6 * SLOT, 8 * SLOT, 7 * SLOT, 0 * SLOT, 9 * SLOT, 5 * SLOT};
    constexpr int ringi[6] = {6, 8, 7, 0, 9, 5};
#define OOFF(ks) (((ks) >> 2) * 28672 + ((ks) & 3) * 512)
    Frags fa, fb;
    load_w(fa, wrd + ring[0]); load_x<4096>(fa, ord + OOFF(0), four);
#pragma unroll
    for (int pass = 0; pass < 2; ++pass) {
      f32x4 acc[4][4];
#pragma unroll
      for (int a = 0; a < 4; ++a)
#pragma unroll
        for (int p = 0; p < 4; ++p) acc[a][p] = f32x4{0.f, 0.f, 0.f, 0.f};
#pragma unroll
      for (int jj = 0; jj < 4; ++jj) {
        const int j = pass * 4 + jj;
        load_w(fb, wrd + ring[(2 * j + 1) % 6]); load_x<4096>(fb, ord + OOFF(2 * jj + 1), four);
        mma_step(acc, fa, four);
        if (j == 4 || j == 5) { if (four) WAIT_BAR(12); else WAIT_BAR(10); }
        else if (j == 7) WAIT_BAR(0);
        else WAIT_BAR(4);
        if (j < 5) { ISSUE_WO(2 * j + 6, ringi[(2 * j) % 6]); ISSUE_WO(2 * j + 7, ringi[(2 * j + 1) % 6]); }
        else if (j == 5) { ISSUE_WO3(16, ringi[(2 * j) % 6]); ISSUE_WO3(17, ringi[(2 * j + 1) % 6]); }
        if (j < 7) { load_w(fa, wrd + ring[(2 * j + 2) % 6]); load_x<4096>(fa, ord + OOFF((2 * jj + 2) & 7), four); }
        mma_step(acc, fb, four);
      }
#pragma unroll
      for (int aa = 0; aa < 2; ++aa)
#pragma unroll
        for (int p = 0; p < 4; ++p) {
          if (p < 3 || four) {
            const f32x4 c0 = acc[2 * aa][p], c1 = acc[2 * aa + 1][p];
            u32x4 v = {cvtpk(c0[0], c0[1]), cvtpk(c0[2], c0[3]), cvtpk(c1[0], c1[1]), cvtpk(c1[2], c1[3])};
            *(u32x4*)(part + (pass * 56 + (nq * 2 + aa) * 7 + p0 + p) * 1024 + lane * 16) = v;
          }
        }
    }
    {
      f32x4 acc3[4];
#pragma unroll
      for (int p = 0; p < 4; ++p) acc3[p] = f32x4{0.f, 0.f, 0.f, 0.f};
      const unsigned w3rd = L0 + (g4 >> 1) * 2048 + ((g4 & 1) * 4 + tq) * 32 + 8 * tp + nq * 256;
#pragma unroll
      for (int ks = 0; ks < 8; ++ks) {
        const unsigned wa = w3rd + ring[4 + (ks >> 2)] + (ks & 3) * 4096;
        const bf16x8 wf = lds_tr2(wa, wa + 1024);
#pragma unroll
        for (int p = 0; p < 4; ++p) {
          if (p < 3 || four) acc3[p] = MFMA16(wf, lds_b128(ord + OOFF(ks) + p * 4096), acc3[p]);
        }
      }
#pragma unroll
      for (int p = 0; p < 4; ++p) {
        if (p < 3 || four) {
          u32x2 v = {cvtpk(acc3[p][0], acc3[p][1]), cvtpk(acc3[p][2], acc3[p][3])};
          *(u32x2*)(part + 112 * 1024 + (nq * 7 + p0 + p) * 512 + lane * 8) = v;
        }
      }
    }
#undef OOFF
  }
}

DI float bf_lo(unsigned v) { return __uint_as_float(v << 16); }
DI float bf_hi(unsigned v) { return __uint_as_float(v & 0xffff0000u); }

__global__ __launch_bounds__(512) void reduce_kernel(const char* part, char* out) {
  const int lane = threadIdx.x & 63, w = threadIdx.x >> 6;
  const int grp = blockIdx.x >> 4, u = (blockIdx.x & 15) * 8 + w;
  if (u >= 126) return;
  const char* src = part + (size_t)grp * HKV * PART_BYTES + u * 1024 + lane * 16;
  u32x4 v[16];
#pragma unroll
  for (int i = 0; i < 16; ++i) v[i] = *(const u32x4*)(src + (size_t)i * PART_BYTES);
  float s[8];
#pragma unroll
  for (int e = 0; e < 8; ++e) s[e] = 0.f;
#pragma unroll
  for (int i = 0; i < 16; ++i)
#pragma unroll
    for (int e = 0; e < 4; ++e) { s[2 * e] += bf_lo(v[i][e]); s[2 * e + 1] += bf_hi(v[i][e]); }
  const int br = grp >> 3, b = grp & 7;
  int pos0, n0, pos1, n1;
  if (u < 112) {
    const int pass = u / 56, rem = u % 56, na = rem / 7, pt = rem % 7;
    pos0 = pos1 = 16 * pt + (lane & 15);
    n0 = 256 * pass + 32 * na + 4 * (lane >> 4);
    n1 = n0 + 16;
  } else {
    const int t = 2 * (u - 112) + (lane >> 5), nqq = t / 7, pt = t % 7, v0 = 2 * (lane & 31);
    pos0 = 16 * pt + (v0 & 15); pos1 = pos0 + 1;
    n0 = n1 = 512 + 16 * nqq + 4 * (v0 >> 4);
  }
  char* ob = out + (size_t)b * SQ * (2 * DM) * 2 + br * DM * 2;
  u32x2 o0 = {cvtpk(s[0], s[1]), cvtpk(s[2], s[3])}, o1 = {cvtpk(s[4], s[5]), cvtpk(s[6], s[7])};
  *(u32x2*)(ob + (size_t)pos0 * (2 * DM * 2) + n0 * 2) = o0;
  *(u32x2*)(ob + (size_t)pos1 * (2 * DM * 2) + n1 * 2) = o1;
}

extern "C" void kernel_launch(void* const* d_in, const int* in_sizes, int n_in,
                              void* d_out, int out_size, void* d_ws, size_t ws_size,
                              hipStream_t stream) {
  Params P;
  for (int br = 0; br < 2; ++br) {
    P.x[br] = (const char*)d_in[5 * br + 0];
    P.k[br] = (const char*)d_in[5 * br + 1];
    P.v[br] = (const char*)d_in[5 * br + 2];
    P.wq[br] = (const char*)d_in[5 * br + 3];
    P.wo[br] = (const char*)d_in[5 * br + 4];
  }
  P.part = (char*)d_ws;
  P.out = (char*)d_out;
  (void)hipFuncSetAttribute((const void*)fused_kernel, hipFuncAttributeMaxDynamicSharedMemorySize, (int)LDS_BYTES);
  hipLaunchKernelGGL(fused_kernel, dim3(256), dim3(512), LDS_BYTES, stream, P);
  hipLaunchKernelGGL(reduce_kernel, dim3(256), dim3(512), 0, stream, (const char*)d_ws, (char*)d_out);
}
```

```cpp
#include <hip/hip_runtime.h>
#include <hip/hip_bf16.h>
#include <stdint.h>

typedef __bf16 bf16x8 __attribute__((ext_vector_type(8)));
typedef __bf16 bf16x2 __attribute__((ext_vector_type(2)));
typedef short s16x4 __attribute__((ext_vector_type(4)));
typedef float f32x2 __attribute__((ext_vector_type(2)));
typedef float f32x4 __attribute__((ext_vector_type(4)));
typedef float f32x16 __attribute__((ext_vector_type(16)));
typedef unsigned u32x2 __attribute__((ext_vector_type(2)));
typedef unsigned u32x4 __attribute__((ext_vector_type(4)));
typedef __amdgpu_buffer_rsrc_t srd_t;
typedef __attribute__((address_space(1))) unsigned long long gu64;
#define LDS_AS __attribute__((address_space(3)))
#define DI __device__ __forceinline__

constexpr int NB = 8, SQ = 112, DM = 576, HKV = 16, SK = 512, DH = 128, NQ = 4096;
constexpr unsigned SLOT = 16384;
constexpr unsigned LDS_BYTES = 10 * SLOT;
constexpr unsigned XB = 4 * SLOT;
constexpr unsigned QB = 2 * SLOT;
constexpr unsigned OB0 = 8 * SLOT, OB1 = 0;
constexpr unsigned PART_BYTES = 129024;
constexpr unsigned SYNC_OFF = 48u << 20;
constexpr int CODE_WARM_BYTES = 50 * 1024;
constexpr float QSCALE = 0.08838834764831845f * 1.4426950408889634f;
constexpr float SUM_LIMIT = 4096.f;

struct Params {
  const char* x[2]; const char* k[2]; const char* v[2]; const char* wq[2]; const char* wo[2];
  char* part; char* out;
};

template <bool NT, int DL, int DS> DI void dma16(srd_t srd, unsigned voff, unsigned sbase, unsigned dbase) {
  unsigned tmp;
  if (NT) asm volatile("s_add_u32 m0, %1, %5\n\ts_add_u32 %0, %4, %6\n\tbuffer_load_dwordx4 %2, %3, %0 offen nt lds"
                       : "=&s"(tmp) : "s"(dbase), "v"(voff), "s"(srd), "s"(sbase), "i"(DL), "i"(DS) : "m0", "scc", "memory");
  else asm volatile("s_add_u32 m0, %1, %5\n\ts_add_u32 %0, %4, %6\n\tbuffer_load_dwordx4 %2, %3, %0 offen lds"
                    : "=&s"(tmp) : "s"(dbase), "v"(voff), "s"(srd), "s"(sbase), "i"(DL), "i"(DS) : "m0", "scc", "memory");
}
DI s16x4 lds_tr(unsigned a) { return __builtin_amdgcn_ds_read_tr16_b64_v4i16((LDS_AS s16x4*)(a)); }
DI bf16x8 lds_tr2(unsigned a0, unsigned a1) {
  s16x4 lo = lds_tr(a0), hi = lds_tr(a1);
  return __builtin_bit_cast(bf16x8, __builtin_shufflevector(lo, hi, 0, 1, 2, 3, 4, 5, 6, 7));
}
DI bf16x8 lds_b128(unsigned a) { return *(const LDS_AS bf16x8*)(a); }
DI unsigned cvtpk(float lo, float hi) {
  f32x2 v = {lo, hi};
  return __builtin_bit_cast(unsigned, __builtin_convertvector(v, bf16x2));
}
DI void lds_w64(unsigned a, unsigned lo, unsigned hi) { u32x2 v = {lo, hi}; *(LDS_AS u32x2*)(a) = v; }
DI float swap32_max(float v) {
  auto r = __builtin_amdgcn_permlane32_swap(__float_as_uint(v), __float_as_uint(v), false, false);
  return fmaxf(__uint_as_float(r[0]), __uint_as_float(r[1]));
}
DI float swap32_add(float v) {
  auto r = __builtin_amdgcn_permlane32_swap(__float_as_uint(v), __float_as_uint(v), false, false);
  return __uint_as_float(r[0]) + __uint_as_float(r[1]);
}
DI unsigned opaque(unsigned v) { asm volatile("" : "+v"(v)); return v; }
#define MFMA16(a, b, c) __builtin_amdgcn_mfma_f32_16x16x32_bf16(a, b, c, 0, 0, 0)
#define MFMA32(a, b, c) __builtin_amdgcn_mfma_f32_32x32x16_bf16(a, b, c, 0, 0, 0)
#define WAIT_BAR(N) asm volatile("s_waitcnt vmcnt(" #N ") lgkmcnt(0)\n\ts_barrier" ::: "memory")
#define LBAR() asm volatile("s_waitcnt lgkmcnt(0)\n\ts_barrier" ::: "memory")
#define SB() __builtin_amdgcn_sched_barrier(0)

struct Ctx {
  srd_t sx, swq, sk, sv, swo;
  unsigned vX, vWq, vWo, vK0, vK1, vKw, vV;
  unsigned L0, xrd, wrd0, wrd1;
  int lane, w, idx;
  const char* pc;
  gu64* epoch; unsigned long long epochv;
  int rot1, rot2;
};

enum { U_X = 0, U_WQ = 1, U_K = 2, U_V = 3, U_WO3 = 5, U_WO3N = 6 };
template <int TYPE> constexpr DI unsigned piece_off(int j) {
  return TYPE == U_X ? j * (16 * DM * 2)
       : TYPE == U_WQ ? (16 * (j >> 3) + 4 * ((j >> 2) & 1)) * (NQ * 2) + (j & 3) * 128
       : (TYPE == U_K || TYPE == U_V) ? (j >> 1) * 2048 + (j & 1) * 128
       : (32 * (j >> 2) + 16 * ((j >> 1) & 1) + 4 * (j & 1)) * (DM * 2);
}
template <int TYPE> DI unsigned unit_off(const Ctx& C, int idx) {
  return TYPE == U_X ? idx * 64
       : TYPE == U_WQ ? idx * (32 * NQ * 2)
       : (TYPE == U_K || TYPE == U_V) ? idx * 16384
       : (idx & 1) * (128 * DM * 2) + (idx >> 1) * 128;
}
template <int TYPE, int I, int N> DI void pieces_rec(const Ctx& C, unsigned sbase, unsigned dbase) {
  if constexpr (I < N) {
    constexpr int DS = (int)piece_off<TYPE>(I);
    if constexpr (TYPE == U_X) dma16<false, I * 1024, DS>(C.sx, C.vX, sbase, dbase);
    else if constexpr (TYPE == U_WQ) dma16<false, I * 1024, DS>(C.swq, C.vWq, sbase, dbase);
    else if constexpr (TYPE == U_K) dma16<true, I * 1024, DS>(C.sk, N == 2 ? C.vKw : (I & 2) ? C.vK1 : C.vK0, sbase, dbase);
    else if constexpr (TYPE == U_V) dma16<true, I * 1024, DS>(C.sv, C.vV, sbase, dbase);
    else dma16<(TYPE == U_WO3N), I * 1024, DS>(C.swo, C.vWo, sbase, dbase);
    pieces_rec<TYPE, I + 1, N>(C, sbase, dbase);
  }
}
template <int TYPE, int N> DI void pieces(const Ctx& C, int idx, unsigned slotb, int j0) {
  pieces_rec<TYPE, 0, N>(C, unit_off<TYPE>(C, idx) + piece_off<TYPE>(j0), C.L0 + slotb + j0 * 1024);
}

struct Frags { bf16x8 w[4]; bf16x8 x[4]; };

template <int PSTRIDE, bool FOUR> DI void load_one(Frags& f, int i, unsigned wa0, unsigned wa1, unsigned xa) {
  if (i < 4) { const unsigned a = ((i & 1) ? wa1 : wa0) + (i >> 1) * 512; f.w[i] = lds_tr2(a, a + 4096); }
  else if (i < 7 || FOUR) f.x[i - 4] = lds_b128(xa + (i - 4) * PSTRIDE);
}
template <int PSTRIDE, bool FOUR, bool LOAD, class Hook>
DI void gemm_step(const Ctx& C, f32x4 (&acc)[4][4], const Frags& cur, Frags& nxt, unsigned wo, unsigned xa, Hook hook) {
  constexpr int NM = FOUR ? 16 : 12;
  unsigned wa0 = 0, wa1 = 0;
  if (LOAD) { wa0 = opaque(C.wrd0 + wo); wa1 = opaque(C.wrd1 + wo); xa = opaque(xa); }
#pragma unroll
  for (int i = 0; i < NM; ++i) {
    acc[i & 3][i >> 2] = MFMA16(cur.w[i & 3], cur.x[i >> 2], acc[i & 3][i >> 2]);
    if (LOAD && i < 8) load_one<PSTRIDE, FOUR>(nxt, i, wa0, wa1, xa);
    hook(i);
    SB();
  }
}

struct G1Dma { srd_t srd; unsigned voff[5], koff, kstep, kend, dbase, dstep; };
template <int BUF, int Q, bool LAST, bool NT> DI void g1_piece(G1Dma& D) {
  dma16<NT, Q * 1024, 0>(D.srd, D.voff[Q], D.koff, D.dbase + BUF * D.dstep);
  if constexpr (LAST) {
    const unsigned n = D.koff + D.kstep;
    D.koff = (n == D.kend) ? n - 16 * D.kstep : n;
  }
}
template <int BUF, int NP, bool NT = false> DI void g1_piece_i(G1Dma& D, int q) {
  if (q == 0) g1_piece<BUF, 0, NP == 1, NT>(D);
  else if (q == 1) g1_piece<BUF, 1, NP == 2, NT>(D);
  else if (q == 2) g1_piece<BUF, 2, NP == 3, NT>(D);
  else if (q == 3) g1_piece<BUF, 3, NP == 4, NT>(D);
  else g1_piece<BUF, 4, NP == 5, NT>(D);
}

template <int N> DI void wait_bar_n() {
  asm volatile("s_waitcnt vmcnt(%0) lgkmcnt(0)\n\ts_barrier" :: "i"(N) : "memory");
}
template <int ROLE, int B, bool LOAD, bool ISSUE, int KV>
DI void g1_step(Ctx& C, G1Dma& D, f32x4 (&acc)[4][4], Frags& cur, Frags& nxt) {
  constexpr bool FOUR = ROLE == 0;
  constexpr int NB_ = (B + 1) % 4, NP = ROLE == 0 ? 2 : 5;
  gemm_step<1024, FOUR, LOAD>(C, acc, cur, nxt, NB_ * SLOT, C.xrd + NB_ * 8192, [&](int i) {
    if constexpr (ROLE < 2 && ISSUE) { if ((i & 1) && (i >> 1) < NP) g1_piece_i<B, NP>(D, i >> 1); }
    if constexpr (ROLE == 2 && KV == 1) {
      if (i == 1 || i == 5) {
        if constexpr (B < 2) pieces<U_K, 4>(C, 0, 6 * SLOT, 8 * B + (i - 1));
        else pieces<U_V, 4>(C, 0, 7 * SLOT, 8 * (B - 2) + (i - 1));
      }
    }
    if constexpr (KV >= 2) {
      if (i == 1) {
        if constexpr (KV == 2) pieces<U_K, 2>(C, 1, 8 * SLOT, 2 * C.w);
        else if constexpr (KV == 3) pieces<U_V, 2>(C, 1, 9 * SLOT, 2 * C.w);
        else if constexpr (KV == 4) pieces<U_K, 2>(C, 2, 0, 2 * C.w);
        else pieces<U_V, 2>(C, 2, SLOT, 2 * C.w);
      }
    }
  });
}
template <int ROLE, int NSTEPS, int EXTRA = 0> DI void g1_bar() {
  if constexpr (ROLE == 2) LBAR(); else wait_bar_n<NSTEPS * (ROLE == 0 ? 2 : 5) + EXTRA>();
}
template <int ROLE, int KV> DI void g1_four(Ctx& C, G1Dma& D, f32x4 (&acc)[4][4], Frags& fa, Frags& fb) {
  g1_step<ROLE, 0, true, true, KV>(C, D, acc, fa, fb); g1_bar<ROLE, 2>();
  g1_step<ROLE, 1, true, true, KV>(C, D, acc, fb, fa); g1_bar<ROLE, 2>();
  g1_step<ROLE, 2, true, true, KV>(C, D, acc, fa, fb); g1_bar<ROLE, 2>();
  g1_step<ROLE, 3, true, true, KV>(C, D, acc, fb, fa); g1_bar<ROLE, 2>();
}
DI void g1_setup(Ctx& C, G1Dma& D, const char* xg, const char* wqg, int wqbytes) {
  const int w = C.w;
  const bool isx = (0x48 >> w) & 1;
  const int g0 = (0x02B60420u >> (4 * w)) & 15;
  const unsigned xm = isx ? 0xffffffffu : 0u;
  const unsigned vl = (C.vX & xm) | (C.vWq & ~xm);
  D.srd = __builtin_amdgcn_make_buffer_rsrc((void*)(isx ? xg : wqg), (short)0, isx ? SQ * DM * 2 : wqbytes, 0x00020000);
#pragma unroll
  for (int q = 0; q < 5; ++q) D.voff[q] = vl + ((piece_off<U_X>(g0 + q) & xm) | (piece_off<U_WQ>((g0 + q) & 15) & ~xm));
  D.kstep = isx ? 64 : 32 * NQ * 2;
  D.koff = 0;
  D.kend = 18 * D.kstep;
  D.dbase = C.L0 + (isx ? XB : 0) + g0 * 1024;
  D.dstep = isx ? 8192 : SLOT;
}
template <int ROLE> DI void g1_phase(Ctx& C, G1Dma& D) {
  constexpr bool FOUR = ROLE == 0;
  constexpr int NP = ROLE == 0 ? 2 : 5;
  const int lane = C.lane, w = C.w, nq = w & 3, p0 = FOUR ? 0 : 4;
  const int g4 = lane >> 4, r16 = lane & 15;
  if constexpr (ROLE < 2) {
#pragma unroll
    for (int q = 0; q < NP; ++q) g1_piece_i<0, NP, false>(D, q);
#pragma unroll
    for (int q = 0; q < NP; ++q) g1_piece_i<1, NP, false>(D, q);
  }
  if constexpr (ROLE < 2) D.koff += C.rot1 * D.kstep;
  asm volatile("s_barrier" ::: "memory");
  if constexpr (ROLE < 2) {
#pragma unroll
    for (int q = 0; q < NP; ++q) g1_piece_i<2, NP, true>(D, q);
#pragma unroll
    for (int q = 0; q < NP; ++q) g1_piece_i<3, NP, true>(D, q);
  }
  u32x4 cw0 = {0u, 0u, 0u, 0u}, cw1 = {0u, 0u, 0u, 0u};
  if constexpr (ROLE == 2) {
    const srd_t sc = __builtin_amdgcn_make_buffer_rsrc((void*)C.pc, (short)0, CODE_WARM_BYTES, 0x00020000);
    cw0 = __builtin_amdgcn_raw_buffer_load_b128(sc, lane * 16, C.idx * 2048, 0);
    cw1 = __builtin_amdgcn_raw_buffer_load_b128(sc, lane * 16, C.idx * 2048 + 1024, 0);
    C.epochv = __hip_atomic_load(C.epoch, __ATOMIC_RELAXED, __HIP_MEMORY_SCOPE_AGENT);
    SB();
  }
  f32x4 acc[4][4];
#pragma unroll
  for (int a = 0; a < 4; ++a)
#pragma unroll
    for (int p = 0; p < 4; ++p) acc[a][p] = f32x4{0.f, 0.f, 0.f, 0.f};
  g1_bar<ROLE, 2>();
  Frags fa, fb;
#pragma unroll
  for (int i = 0; i < 8; ++i) load_one<1024, FOUR>(fa, i, C.wrd0, C.wrd1, C.xrd);
  LBAR();
  for (int it = 0; it < 3; ++it) {
    if (ROLE == 2 && it == 1) g1_four<ROLE, 1>(C, D, acc, fa, fb);
    else g1_four<ROLE, 0>(C, D, acc, fa, fb);
  }
  if constexpr (ROLE == 2) asm volatile("" : "+v"(C.epochv) : "v"(cw0), "v"(cw1));
  g1_step<ROLE, 0, true, true, 0>(C, D, acc, fa, fb); g1_bar<ROLE, 2>();
  g1_step<ROLE, 1, true, true, 0>(C, D, acc, fb, fa); g1_bar<ROLE, 2>();
  g1_step<ROLE, 2, true, false, 2>(C, D, acc, fa, fb); g1_bar<ROLE, 1, 2>();
  g1_step<ROLE, 3, true, false, 3>(C, D, acc, fb, fa); g1_bar<ROLE, 0, 4>();
  g1_step<ROLE, 0, true, false, 4>(C, D, acc, fa, fb); LBAR();
  g1_step<ROLE, 1, false, false, 5>(C, D, acc, fb, fa);
  const unsigned qw = C.L0 + QB + ((nq >> 1) * 14 + 2 * p0 + (r16 >> 3)) * 2048 + (r16 & 7) * 16 + 8 * (g4 & 1);
#pragma unroll
  for (int a = 0; a < 4; ++a) {
    const unsigned ch = (unsigned)((8 * (nq & 1) + 2 * a + (g4 >> 1)) ^ (r16 >> 3)) * 128;
#pragma unroll
    for (int p = 0; p < (FOUR ? 4 : 3); ++p) {
      const f32x4 c = acc[a][p] * QSCALE;
      lds_w64(qw + ch + p * 4096, cvtpk(c[0], c[1]), cvtpk(c[2], c[3]));
    }
  }
}

struct Att {
  bf16x8 qf[8]; bf16x8 fr[8]; f32x16 o[4]; f32x16 negm; u32x4 pw[4];
  float mref, l;
  unsigned krd0, krd1, vrd;
};
constexpr int PD = 6;
struct KA { unsigned a0, a1; };
DI bf16x8 kfrag(const KA& ka, int i) { return lds_b128((((i >> 1) & 1) ? ka.a1 : ka.a0) + (i >> 2) * 512 + (i & 1) * 8192); }
DI bf16x8 vfrag(unsigned va, int i) {
  const unsigned a = va + (i & 3) * 4096 + (i >> 2) * 512;
  return lds_tr2(a, a + 2048);
}
DI unsigned kslot(int t) { return ((0x08642086u >> (4 * t)) & 15u) * SLOT; }
DI void att_issue(const Ctx& C, int t) {
  if (t < 5) { const unsigned sl = kslot(t + 3); pieces<U_K, 16>(C, t + 3, sl, 0); pieces<U_V, 16>(C, t + 3, sl + SLOT, 0); }
  else {
    int g = t - 5 + C.rot2; g = g >= 9 ? g - 9 : g;
    if (t == 5) {
      pieces<U_WO3N, 16>(C, 2 * g, 2 * SLOT, 0);
      pieces<U_WO3N, 16>(C, 2 * g + 1, 4 * SLOT, 0);
    } else {
      pieces<U_WO3, 16>(C, 2 * g, (t == 6 ? 3 : 5) * SLOT, 0);
      pieces<U_WO3, 16>(C, 2 * g + 1, (t == 6 ? 6 : 7) * SLOT, 0);
    }
  }
}
DI void att_qk(Att& A, f32x16& s0, f32x16& s1, const KA& ka, unsigned va) {
#pragma unroll
  for (int i = 0; i < 16; ++i) {
    if (i == 0) s0 = MFMA32(A.fr[0], A.qf[0], A.negm);
    else if (i == 1) s1 = MFMA32(A.fr[1], A.qf[0], A.negm);
    else if (i & 1) s1 = MFMA32(A.fr[i & 7], A.qf[i >> 1], s1);
    else s0 = MFMA32(A.fr[i & 7], A.qf[i >> 1], s0);
    const int j = i + PD;
    A.fr[j & 7] = (j < 16) ? kfrag(ka, j) : vfrag(va, j - 16);
    SB();
  }
}
template <bool TAILK> DI void att_pv(Att& A, unsigned va, const KA& ka) {
#pragma unroll
  for (int i = 0; i < 16; ++i) {
    A.o[i >> 2] = MFMA32(A.fr[i & 7], __builtin_bit_cast(bf16x8, A.pw[i & 3]), A.o[i >> 2]);
    const int j = i + PD;
    if (j < 16) A.fr[j & 7] = vfrag(va, j);
    else if (TAILK) A.fr[j & 7] = kfrag(ka, j - 16);
    SB();
  }
}
DI float att_exp(Att& A, const f32x16& s0, const f32x16& s1) {
  float ps = 0.f;
#pragma unroll
  for (int i = 0; i < 8; ++i) {
    const float a0 = __builtin_amdgcn_exp2f(s0[2 * i]), a1 = __builtin_amdgcn_exp2f(s0[2 * i + 1]);
    const float b0 = __builtin_amdgcn_exp2f(s1[2 * i]), b1 = __builtin_amdgcn_exp2f(s1[2 * i + 1]);
    ps += a0; ps += a1; ps += b0; ps += b1;
    A.pw[i >> 2][i & 3] = cvtpk(a0, a1);
    A.pw[2 + (i >> 2)][i & 3] = cvtpk(b0, b1);
    SB();
  }
  return ps;
}
template <bool first> DI void att_move_ref(Att& A, f32x16& s0, f32x16& s1) {
  float d = fmaxf(s0[0], s1[0]);
#pragma unroll
  for (int r = 1; r < 16; ++r) d = fmaxf(d, fmaxf(s0[r], s1[r]));
  d = swap32_max(d);
  const float dl = first ? d : fmaxf(d, 0.f);
  A.mref += dl;
  if (!first) {
    const float f = __builtin_amdgcn_exp2f(-dl);
    A.l *= f;
#pragma unroll
    for (int db = 0; db < 4; ++db)
#pragma unroll
      for (int r = 0; r < 16; ++r) A.o[db][r] *= f;
  }
#pragma unroll
  for (int r = 0; r < 16; ++r) { s0[r] -= dl; s1[r] -= dl; A.negm[r] = -A.mref; }
}
DI void att_softmax(Att& A, f32x16& s0, f32x16& s1) {
  float ps = att_exp(A, s0, s1);
  if (__builtin_expect(__any(!(ps <= SUM_LIMIT)), 0)) {
    att_move_ref<false>(A, s0, s1);
    ps = att_exp(A, s0, s1);
  }
  A.l += ps;
  SB();
}

template <int ROLE> DI void att_phase(Ctx& C) {
  const int lane = C.lane, w = C.w;
  const int r32 = lane & 31, h = lane >> 5;
  Att A;
  if constexpr (ROLE < 2) {
    A.krd0 = C.L0 + (r32 >> 3) * 2048 + (r32 & 7) * 64 + (unsigned)(h ^ ((r32 >> 2) & 3)) * 16;
    A.krd1 = C.L0 + (r32 >> 3) * 2048 + (r32 & 7) * 64 + (unsigned)((2 + h) ^ ((r32 >> 2) & 3)) * 16;
    A.vrd = C.L0 + (4 * h + ((lane & 15) >> 2)) * 64 + 32 * ((lane >> 4) & 1) + 8 * (lane & 3);
    const unsigned qa = C.L0 + QB + w * 8192 + (r32 >> 3) * 2048 + (unsigned)(h ^ ((r32 >> 3) & 1)) * 128 + (r32 & 7) * 16;
#pragma unroll
    for (int s = 0; s < 8; ++s) A.qf[s] = lds_b128(qa + s * 256);
#pragma unroll
    for (int d = 0; d < 4; ++d)
#pragma unroll
      for (int r = 0; r < 16; ++r) A.o[d][r] = 0.f;
#pragma unroll
    for (int r = 0; r < 16; ++r) A.negm[r] = 0.f;
    A.mref = 0.f; A.l = 0.f;
  }
  unsigned va = 0;
  f32x16 s0, s1;
  WAIT_BAR(8);
  if constexpr (ROLE < 2) {
    const KA ka = {opaque(A.krd0 + 6 * SLOT), opaque(A.krd1 + 6 * SLOT)};
    va = opaque(A.vrd + 7 * SLOT);
#pragma unroll
    for (int i = 0; i < PD; ++i) A.fr[i] = kfrag(ka, i);
    SB();
    att_qk(A, s0, s1, ka, va);
    att_move_ref<true>(A, s0, s1);
    if constexpr (ROLE == 1) { A.l += att_exp(A, s0, s1); SB(); }
  } else {
    att_issue(C, 0);
  }
  for (int t = 1; t < 8; ++t) {
    const unsigned ksl = kslot(t), vsl = ksl + SLOT;
    if constexpr (ROLE == 2) { if (t == 1) WAIT_BAR(36); else WAIT_BAR(63); } else WAIT_BAR(0);
    if constexpr (ROLE < 2) {
      if constexpr (ROLE == 0) att_softmax(A, s0, s1);
      __builtin_amdgcn_s_setprio(1 + ROLE);
      const KA ka = {opaque(A.krd0 + ksl), opaque(A.krd1 + ksl)};
      att_pv<true>(A, va, ka);
      va = opaque(A.vrd + vsl);
      att_qk(A, s0, s1, ka, va);
      __builtin_amdgcn_s_setprio(0);
      SB();
      if constexpr (ROLE == 1) att_softmax(A, s0, s1);
    } else {
      att_issue(C, t);
    }
  }
  if constexpr (ROLE < 2) {
    if constexpr (ROLE == 0) att_softmax(A, s0, s1);
    __builtin_amdgcn_s_setprio(1 + ROLE);
    const KA ka = {0u, 0u};
    att_pv<false>(A, va, ka);
    __builtin_amdgcn_s_setprio(0);
  }
  LBAR();
  if constexpr (ROLE < 2) {
    const float inv = 1.f / swap32_add(A.l);
    const int R = 32 * w + r32, hd = R >= SQ, pos = R - hd * SQ;
    const unsigned ow = C.L0 + (hd ? OB1 : OB0) + (pos >> 3) * 2048 + (pos & 7) * 16 + 8 * h;
#pragma unroll
    for (int db = 0; db < 4; ++db)
#pragma unroll
      for (int i = 0; i < 4; ++i)
        lds_w64(ow + (4 * db + i) * 128, cvtpk(A.o[db][4 * i] * inv, A.o[db][4 * i + 1] * inv),
                cvtpk(A.o[db][4 * i + 2] * inv, A.o[db][4 * i + 3] * inv));
  }
}

template <bool FOUR> struct G2 {
  static constexpr int NPT = FOUR ? 4 : 3;
  bf16x8 of[NPT][8]; bf16x8 wf[2][8]; f32x4 acc[NPT]; u32x4 pk[2];
  unsigned w3rd, sbase;
};
constexpr unsigned g2_slot(int step, int half) {
  return ((step % 5) == 0 ? (half ? 4 : 2) : (step % 5) == 1 ? (half ? 6 : 3) : (step % 5) == 2 ? (half ? 7 : 5) : (step % 5) == 3 ? (half ? 9 : 8) : (half ? 1 : 0)) * SLOT;
}
template <bool FOUR> DI void g2_pack(G2<FOUR>& G) {
  G.pk[0] = u32x4{cvtpk(G.acc[0][0], G.acc[0][1]), cvtpk(G.acc[0][2], G.acc[0][3]), cvtpk(G.acc[1][0], G.acc[1][1]), cvtpk(G.acc[1][2], G.acc[1][3])};
  if constexpr (FOUR) G.pk[1] = u32x4{cvtpk(G.acc[2][0], G.acc[2][1]), cvtpk(G.acc[2][2], G.acc[2][3]), cvtpk(G.acc[3][0], G.acc[3][1]), cvtpk(G.acc[3][2], G.acc[3][3])};
  else {
    const unsigned a = cvtpk(G.acc[2][0], G.acc[2][1]), b = cvtpk(G.acc[2][2], G.acc[2][3]);
    auto ra = __builtin_amdgcn_permlane32_swap(a, a, false, false);
    auto rb = __builtin_amdgcn_permlane32_swap(b, b, false, false);
    G.pk[1] = u32x4{ra[0], rb[0], ra[1], rb[1]};
  }
}
template <bool FOUR> DI void g2_store(const Ctx& C, G2<FOUR>& G, srd_t sp, int g, int q) {
  const int nq = C.w & 3;
  if (FOUR || q == 0) __builtin_amdgcn_raw_buffer_store_b128(G.pk[q], sp, opaque(G.sbase + g * 14336 + q * 1024), 0, 16);
  else {
    const unsigned off = C.lane < 32 ? G.sbase + g * 14336 + (4 - nq) * 1024 + nq * 512 : 0x7ff00000u;
    __builtin_amdgcn_raw_buffer_store_b128(G.pk[1], sp, opaque(off), 0, 16);
  }
}
template <bool FOUR, int I> DI void g2_steps(Ctx& C, G2<FOUR>& G, srd_t sp, gu64* flag, unsigned long long tag16) {
  if constexpr (I < 9) {
    constexpr int NPT = G2<FOUR>::NPT, CUR = I & 1;
    const int nq = C.w & 3;
    int gprev = I - 1 + C.rot2; gprev = gprev >= 9 ? gprev - 9 : gprev;
    int gnext = I + 3 + C.rot2; gnext = gnext >= 9 ? gnext - 9 : gnext;
#pragma unroll
    for (int p = 0; p < NPT; ++p) G.acc[p] = f32x4{0.f, 0.f, 0.f, 0.f};
#pragma unroll
    for (int ks = 0; ks < 4; ++ks) {
#pragma unroll
      for (int p = 0; p < NPT; ++p) { G.acc[p] = MFMA16(G.wf[CUR][ks], G.of[p][ks], G.acc[p]); SB(); }
      if (I > 0 && (ks == 0 || ks == 2)) { g2_store<FOUR>(C, G, sp, gprev, ks >> 1); SB(); }
    }
    if constexpr (I < 8) {
      if constexpr (FOUR) wait_bar_n<4>();
      else wait_bar_n<(I == 0 ? 32 : I == 1 ? 10 : I <= 6 ? 12 : 4)>();
      if constexpr (FOUR && I >= 3) { if (C.w == 0 && C.lane == 0) __hip_atomic_store(flag, tag16 + (I - 2), __ATOMIC_RELAXED, __HIP_MEMORY_SCOPE_AGENT); }
    }
    const unsigned wa0 = opaque(G.w3rd + g2_slot(I + 1, 0)), wa1 = opaque(G.w3rd + g2_slot(I + 1, 1));
#pragma unroll
    for (int ks = 4; ks < 8; ++ks) {
#pragma unroll
      for (int p = 0; p < NPT; ++p) {
        G.acc[p] = MFMA16(G.wf[CUR][ks], G.of[p][ks], G.acc[p]);
        if constexpr (I < 8) {
          if (p < 2) { const int f = 2 * (ks - 4) + p; const unsigned a = ((f >> 2) ? wa1 : wa0) + (f & 3) * 4096; G.wf[CUR ^ 1][f] = lds_tr2(a, a + 1024); }
        }
        if constexpr (!FOUR && I <= 5) {
          if (p >= 1) { const int f = 2 * (ks - 4) + p - 1; pieces<U_WO3, 1>(C, 2 * gnext + (f >> 2), g2_slot(I + 3, f >> 2), 4 * nq + (f & 3)); }
        }
        SB();
      }
    }
    g2_pack<FOUR>(G);
    SB();
    g2_steps<FOUR, I + 1>(C, G, sp, flag, tag16);
  }
}
template <bool FOUR> DI void g2_phase(Ctx& C, srd_t sp, unsigned pbase, gu64* flag, unsigned long long tag16) {
  constexpr int NPT = G2<FOUR>::NPT;
  const int lane = C.lane, nq = C.w & 3, p0 = FOUR ? 0 : 4;
  const int g4 = lane >> 4, r16 = lane & 15;
  G2<FOUR> G;
  G.w3rd = C.L0 + (g4 >> 1) * 2048 + (nq >> 1) * 512 + ((g4 & 1) * 4 + (r16 >> 2)) * 64 + (unsigned)((nq & 1) ^ (g4 & 1)) * 32 + 8 * (lane & 3);
  G.sbase = lane * 16 + pbase + (FOUR ? nq * 2 : 8 + nq) * 1024;
  {
    const unsigned wa0 = opaque(G.w3rd + g2_slot(0, 0)), wa1 = opaque(G.w3rd + g2_slot(0, 1));
    const unsigned o0 = opaque(C.L0 + OB0 + (r16 >> 3) * 2048 + g4 * 128 + (r16 & 7) * 16 + p0 * 4096), o1 = opaque(o0 - OB0 + OB1);
#pragma unroll
    for (int ks = 0; ks < 8; ++ks) {
      const unsigned a = ((ks >> 2) ? wa1 : wa0) + (ks & 3) * 4096;
      G.wf[0][ks] = lds_tr2(a, a + 1024);
#pragma unroll
      for (int p = 0; p < NPT; ++p) G.of[p][ks] = lds_b128(((ks >> 2) ? o1 : o0) + (ks & 3) * 512 + p * 4096);
    }
  }
  SB();
  g2_steps<FOUR, 0>(C, G, sp, flag, tag16);
  int g = 8 + C.rot2; g = g >= 9 ? g - 9 : g;
  g2_store<FOUR>(C, G, sp, g, 0);
  g2_store<FOUR>(C, G, sp, g, 1);
}

DI float bf_lo(unsigned v) { return __uint_as_float(v << 16); }
DI float bf_hi(unsigned v) { return __uint_as_float(v & 0xffff0000u); }

DI void wait_flags(gu64* flags, unsigned long long tag16, unsigned need, int lane) {
  unsigned spins = 0;
  for (;;) {
    const unsigned long long f = __hip_atomic_load(flags + (lane & 15) * 8, __ATOMIC_RELAXED, __HIP_MEMORY_SCOPE_AGENT) - tag16;
    if (__all(f >= need && f <= 9ull) || ++spins > (1u << 22)) break;
    __builtin_amdgcn_s_sleep(2);
  }
  __builtin_amdgcn_fence(__ATOMIC_ACQUIRE, "wavefront");
}
DI void reduce_load(u32x4 (&v)[16], srd_t sp, int u, int lane) {
#pragma unroll
  for (int i = 0; i < 16; ++i) v[i] = __builtin_amdgcn_raw_buffer_load_b128(sp, lane * 16, i * PART_BYTES + u * 1024, 16);
}
DI void reduce_finish(const u32x4 (&v)[16], char* out, int u, int lane, int br, int b) {
  float s[8];
#pragma unroll
  for (int e = 0; e < 8; ++e) s[e] = 0.f;
#pragma unroll
  for (int i = 0; i < 16; ++i)
#pragma unroll
    for (int e = 0; e < 4; ++e) { s[2 * e] += bf_lo(v[i][e]); s[2 * e + 1] += bf_hi(v[i][e]); }
  const int g = u / 14, q = u % 14;
  int pos0, n0, pos1, n1;
  if (q < 12) {
    const int nq = q < 8 ? q >> 1 : q - 8;
    pos0 = (q < 8 ? 32 * (q & 1) : 64) + (lane & 15); pos1 = pos0 + 16;
    n0 = n1 = 64 * g + 16 * nq + 4 * (lane >> 4);
  } else {
    pos0 = pos1 = 96 + (lane & 15);
    n0 = 64 * g + 16 * (2 * (q - 12) + (lane >> 5)) + 4 * ((lane >> 4) & 1); n1 = n0 + 8;
  }
  char* ob = out + (size_t)b * SQ * (2 * DM) * 2 + br * DM * 2;
  u32x2 o0 = {cvtpk(s[0], s[1]), cvtpk(s[2], s[3])}, o1 = {cvtpk(s[4], s[5]), cvtpk(s[6], s[7])};
  *(u32x2*)(ob + (size_t)pos0 * (2 * DM * 2) + n0 * 2) = o0;
  *(u32x2*)(ob + (size_t)pos1 * (2 * DM * 2) + n1 * 2) = o1;
}

__global__ __launch_bounds__(512, 2) void fused_kernel(Params P) {
  extern __shared__ __attribute__((aligned(16))) char smem[];
  Ctx C;
  C.L0 = (unsigned)(uintptr_t)(LDS_AS char*)smem;
  const int tid = threadIdx.x, lane = tid & 63;
  const int w = __builtin_amdgcn_readfirstlane(tid >> 6);
  C.lane = lane; C.w = w; C.idx = blockIdx.x >> 3;
  asm volatile("s_getpc_b64 %0" : "=s"(C.pc));
  asm volatile("" : "+s"(P.part), "+s"(P.out), "+s"(P.wo[0]), "+s"(P.wo[1]));
  const int bid = blockIdx.x, xcd = bid & 7, idx = bid >> 3;
  const int br = xcd >> 2, kvh = 4 * (xcd & 3) + (idx & 3), b = idx >> 2;
  C.rot1 = 2 * ((b + 2 * (idx & 3)) & 7); C.rot2 = b;

  const int xb = b, wk = kvh;
  C.sx = __builtin_amdgcn_make_buffer_rsrc((void*)((br ? P.x[1] : P.x[0]) + (size_t)xb * SQ * DM * 2), (short)0, SQ * DM * 2, 0x00020000);
  C.swq = __builtin_amdgcn_make_buffer_rsrc((void*)((br ? P.wq[1] : P.wq[0]) + (size_t)wk * 512), (short)0, DM * NQ * 2 - wk * 512, 0x00020000);
  const int kvsel = b * HKV + kvh;
  C.sk = __builtin_amdgcn_make_buffer_rsrc((void*)((br ? P.k[1] : P.k[0]) + (size_t)kvsel * SK * DH * 2), (short)0, SK * DH * 2, 0x00020000);
  C.sv = __builtin_amdgcn_make_buffer_rsrc((void*)((br ? P.v[1] : P.v[0]) + (size_t)kvsel * SK * DH * 2), (short)0, SK * DH * 2, 0x00020000);
  C.swo = __builtin_amdgcn_make_buffer_rsrc((void*)((br ? P.wo[1] : P.wo[0]) + (size_t)kvh * 256 * DM * 2), (short)0, 256 * DM * 2, 0x00020000);

  const int grp = br * NB + b;
  gu64* epoch = (gu64*)(P.part + SYNC_OFF + grp * 256);
  gu64* flags = (gu64*)(P.part + SYNC_OFF + 4096 + grp * (HKV * 64));
  C.epoch = epoch; C.epochv = 0;

  C.vX = (lane >> 2) * (DM * 2) + (unsigned)((lane & 3) ^ (3 * (lane >> 5))) * 16;
  const int r8 = (lane >> 2) & 7, wrow = (r8 & 3) + 8 * (r8 >> 2);
  const unsigned wcol = (lane >> 5) * 64 + (unsigned)(((lane >> 1) & 1) ^ (r8 >> 2)) * 32 + (lane & 1) * 16;
  C.vWq = wrow * (NQ * 2) + wcol; C.vWo = wrow * (DM * 2) + wcol;
  C.vK0 = r8 * 256 + (lane >> 5) * 64 + (unsigned)((lane & 3) ^ (r8 >> 2)) * 16;
  C.vK1 = r8 * 256 + (lane >> 5) * 64 + (unsigned)((lane & 3) ^ (2 + (r8 >> 2))) * 16;
  C.vKw = (w & 1) ? C.vK1 : C.vK0;
  C.vV = r8 * 256 + (lane >> 5) * 64 + (lane & 3) * 16;
  const int g4 = lane >> 4, r16 = lane & 15;
  C.xrd = C.L0 + XB + r16 * 64 + (unsigned)(g4 ^ (3 * (r16 >> 3))) * 16 + (w >> 2) * 4 * 1024;
  C.wrd0 = C.L0 + (g4 >> 1) * 8192 + (w & 3) * 1024 + ((g4 & 1) * 4 + (r16 >> 2)) * 64 + (g4 & 1) * 32 + 8 * (lane & 3);
  C.wrd1 = C.wrd0 ^ 32u;

  {
    const char* xg = (br ? P.x[1] : P.x[0]) + (size_t)xb * SQ * DM * 2;
    const char* wqg = (br ? P.wq[1] : P.wq[0]) + (size_t)wk * 512;
    G1Dma D;
    g1_setup(C, D, xg, wqg, DM * NQ * 2 - wk * 512);
    if (w < 4) g1_phase<0>(C, D); else if (w < 7) g1_phase<1>(C, D); else g1_phase<2>(C, D);
  }
  LBAR();
  if (w < 4) att_phase<0>(C); else if (w < 7) att_phase<1>(C);
  else { att_phase<2>(C); lds_w64(C.L0 + OB0 + 28672, (unsigned)C.epochv, (unsigned)(C.epochv >> 32)); }
  WAIT_BAR(63);
  const unsigned long long tag = *(const LDS_AS unsigned long long*)(C.L0 + OB0 + 28672) + 1ull;
  const srd_t sp = __builtin_amdgcn_make_buffer_rsrc((void*)(P.part + (size_t)grp * HKV * PART_BYTES), (short)0, HKV * PART_BYTES, 0x00020000);
  const unsigned long long tag16 = tag << 4;
  if (w < 4) g2_phase<true>(C, sp, kvh * PART_BYTES, flags + kvh * 8, tag16); else g2_phase<false>(C, sp, kvh * PART_BYTES, flags + kvh * 8, tag16);
  WAIT_BAR(0);
  if (tid == 0) __hip_atomic_store(flags + kvh * 8, tag16 + 9, __ATOMIC_RELAXED, __HIP_MEMORY_SCOPE_AGENT);
  const int uo = w * 16 + kvh;
  if (uo < 126) {
    const int st = uo / 14;
    int g = st + C.rot2; g = g >= 9 ? g - 9 : g;
    const int u = g * 14 + uo % 14;
    u32x4 v[16];
    wait_flags(flags, tag16, st + 1, lane);
    reduce_load(v, sp, u, lane);
    reduce_finish(v, P.out, u, lane, br, b);
    if (kvh == 0 && tid == 0) __hip_atomic_store(epoch, tag, __ATOMIC_RELAXED, __HIP_MEMORY_SCOPE_AGENT);
  }
}

extern "C" void kernel_launch(void* const* d_in, const int* in_sizes, int n_in,
                              void* d_out, int out_size, void* d_ws, size_t ws_size,
                              hipStream_t stream) {
  Params P;
  for (int br = 0; br < 2; ++br) {
    P.x[br] = (const char*)d_in[5 * br + 0];
    P.k[br] = (const char*)d_in[5 * br + 1];
    P.v[br] = (const char*)d_in[5 * br + 2];
    P.wq[br] = (const char*)d_in[5 * br + 3];
    P.wo[br] = (const char*)d_in[5 * br + 4];
  }
  P.part = (char*)d_ws + ((ws_size - (50u << 20)) & ~(size_t)4095);
  P.out = (char*)d_out;
  (void)hipFuncSetAttribute((const void*)fused_kernel, hipFuncAttributeMaxDynamicSharedMemorySize, (int)LDS_BYTES);
  hipLaunchKernelGGL(fused_kernel, dim3(256), dim3(512), LDS_BYTES, stream, P);
}
```
